```python
import torch

def _softmax(x, axis=-1):
    return torch.softmax(x, dim=axis)
def _gelu(x):
    return torch.nn.functional.gelu(x, approximate="tanh")
def _silu(x):
    return torch.nn.functional.silu(x)
def _sigmoid(x):
    return torch.sigmoid(x)
def _layernorm(x, axis=-1, eps=1e-5):
    m = x.mean(dim=axis, keepdim=True)
    v = x.var(dim=axis, keepdim=True, unbiased=False)
    return (x - m) / torch.sqrt(v + eps)
def _scatter_add(tgt, idx, src, axis=0):
    return tgt.clone().index_add_(axis, idx, src)
def _segment_sum(src, seg, n):
    out = torch.zeros((n,) + src.shape[1:], dtype=src.dtype)
    return out.index_add_(0, seg, src)
def _pad(x, pw):
    flat = tuple(v for pair in reversed(pw) for v in pair)
    return torch.nn.functional.pad(x, flat)

kernel_name = 'parallel_sum__mha_block_cached__geglu_ffn__decode'

def setup_inputs():

    _w = (lambda a: (lambda t: t.to(torch.bfloat16) if t.is_floating_point() else t)(torch.as_tensor(a)))
    def _A_setup():
        B, Sq, D = 1, 1, 512
        H, Sk, Dh = 32, 32768, 128
        return {
            "x": _w((lambda *s: torch.randn(*s, dtype=torch.bfloat16) * 0.05)(B, Sq, D)),
            "k_cache": _w((lambda *s: torch.randn(*s, dtype=torch.bfloat16) * 3.2)(B, H, Sk, Dh)),
            "v_cache": _w((lambda *s: torch.randn(*s, dtype=torch.bfloat16) * 1.6)(B, H, Sk, Dh)),
            "wq": _w((lambda *s: torch.randn(*s, dtype=torch.bfloat16) * 3.2)(D, H * Dh) * (D ** -0.5)),
            "wo": _w((lambda *s: torch.randn(*s, dtype=torch.bfloat16) * 1.6)(H * Dh, D) * ((H * Dh) ** -0.5)),
        }
    _A_in = _A_setup()

    def _B_setup():
        B, S, D, F = 1, 1, 512, 1376
        return {
            "x": _w((lambda *s: torch.randn(*s, dtype=torch.bfloat16) * 0.05)(B, S, D)),
            "w_gate": _w((lambda *s: torch.randn(*s, dtype=torch.bfloat16) * 0.05)(D, F)),
            "w_up": _w((lambda *s: torch.randn(*s, dtype=torch.bfloat16) * 0.05)(D, F)),
            "w_down": _w((lambda *s: torch.randn(*s, dtype=torch.bfloat16) * 0.05)(F, D)),
        }
    _B_in = _B_setup()

    return {
        "A_x": _A_in["x"],
        "A_k_cache": _A_in["k_cache"],
        "A_v_cache": _A_in["v_cache"],
        "A_wq": _A_in["wq"],
        "A_wo": _A_in["wo"],
        "B_x": _B_in["x"],
        "B_w_gate": _B_in["w_gate"],
        "B_w_up": _B_in["w_up"],
        "B_w_down": _B_in["w_down"],
    }

def reference(A_x, A_k_cache, A_v_cache, A_wq, A_wo, B_x, B_w_gate, B_w_up, B_w_down):
    def _A_ref(x, k_cache, v_cache, wq, wo):
        B, Sq, D = x.shape
        H, Dh = 32, 128
        q = (lambda x, *d: x.permute(*d))((x @ wq).reshape(B, Sq, H, Dh), 0, 2, 1, 3)
        scores = torch.einsum("bhqd,bhkd->bhqk", q, k_cache) * (Dh ** -0.5)
        p = _softmax(scores, axis=-1)
        o = torch.einsum("bhqk,bhkd->bhqd", p, v_cache)
        return (lambda x, *d: x.permute(*d))(o, 0, 2, 1, 3).reshape(B, Sq, H * Dh) @ wo

    def _B_ref(x, w_gate, w_up, w_down):
        return (_gelu(x @ w_gate) * (x @ w_up)) @ w_down

    x = A_x
    xn = _layernorm(x, axis=-1)
    return x + _A_ref(xn, A_k_cache, A_v_cache, A_wq, A_wo) + _B_ref(xn, B_w_gate, B_w_up, B_w_down)
```

```cpp
#include <hip/hip_runtime.h>
#include <hip/hip_bf16.h>

#define MIMIC 1
#define WIN 5

typedef unsigned v4u __attribute__((ext_vector_type(4)));
typedef float f32x2 __attribute__((ext_vector_type(2)));
typedef float f32x4 __attribute__((ext_vector_type(4)));
typedef __bf16 bf16x2 __attribute__((ext_vector_type(2)));
#define GAS __attribute__((address_space(1)))

constexpr int DM = 512, NH = 32, SK = 32768, DH = 128, FF = 1376;
constexpr int NWG = 256, NFS = FF / 8;
constexpr int NGRP = 16;
constexpr int WS_YA = 0;
constexpr int WS_YF = WS_YA + NWG * DM;
constexpr int WS_LSE = WS_YF + NFS * DM;

__device__ __forceinline__ v4u ldnt(const GAS char* p) { return __builtin_nontemporal_load((const GAS v4u*)p); }
__device__ __forceinline__ v4u ldg(const GAS char* p) { return *(const GAS v4u*)p; }
__device__ __forceinline__ float bflo(unsigned u) { return __builtin_bit_cast(float, u << 16); }
__device__ __forceinline__ float bfhi(unsigned u) { return __builtin_bit_cast(float, u & 0xffff0000u); }
__device__ __forceinline__ float rbf(float x) { return (float)(__bf16)x; }
__device__ __forceinline__ float mrbf(float x) { return MIMIC ? rbf(x) : x; }
__device__ __forceinline__ unsigned packbf(float lo, float hi) {
  bf16x2 v = {(__bf16)lo, (__bf16)hi};
  return __builtin_bit_cast(unsigned, v);
}
__device__ __forceinline__ float dot2(unsigned k, unsigned q, float acc) {
  return __builtin_amdgcn_fdot2_f32_bf16(__builtin_bit_cast(bf16x2, k), __builtin_bit_cast(bf16x2, q), acc, false);
}
template <int CTRL> __device__ __forceinline__ float dpp(float x) {
  return __builtin_bit_cast(float, __builtin_amdgcn_mov_dpp(__builtin_bit_cast(int, x), CTRL, 0xf, 0xf, true));
}
constexpr int XOR1 = 0xB1, XOR2 = 0x4E, XOR7 = 0x141, XOR8 = 0x128;
__device__ __forceinline__ float row_sum(float x) { x += dpp<XOR8>(x); x += dpp<XOR7>(x); x += dpp<XOR2>(x); x += dpp<XOR1>(x); return x; }
__device__ __forceinline__ float row_max(float x) { x = fmaxf(x, dpp<XOR8>(x)); x = fmaxf(x, dpp<XOR7>(x)); x = fmaxf(x, dpp<XOR2>(x)); x = fmaxf(x, dpp<XOR1>(x)); return x; }
__device__ __forceinline__ float xrow_sum(float x) {
  auto s = __builtin_amdgcn_permlane16_swap(__float_as_uint(x), __float_as_uint(x), false, false);
  x = __uint_as_float(s[0]) + __uint_as_float(s[1]);
  auto t = __builtin_amdgcn_permlane32_swap(__float_as_uint(x), __float_as_uint(x), false, false);
  return __uint_as_float(t[0]) + __uint_as_float(t[1]);
}
__device__ __forceinline__ float xrow_max(float x) {
  auto s = __builtin_amdgcn_permlane16_swap(__float_as_uint(x), __float_as_uint(x), false, false);
  x = fmaxf(__uint_as_float(s[0]), __uint_as_float(s[1]));
  auto t = __builtin_amdgcn_permlane32_swap(__float_as_uint(x), __float_as_uint(x), false, false);
  return fmaxf(__uint_as_float(t[0]), __uint_as_float(t[1]));
}
__device__ __forceinline__ float tr_reduce16(float (&x)[16], int c) {
#define TR_STEP(HALF, CTRL, BIT)                                                      \
  _Pragma("unroll") for (int h = 0; h < (HALF); ++h) {                                \
    const float lo_ = x[h], hi_ = x[h + (HALF)];                                      \
    const float t1_ = lo_ + dpp<CTRL>(lo_), t2_ = hi_ + dpp<CTRL>(hi_);               \
    x[h] = (c & (BIT)) ? t2_ : t1_;                                                   \
  }
  TR_STEP(8, XOR8, 8) TR_STEP(4, XOR7, 4) TR_STEP(2, XOR2, 2) TR_STEP(1, XOR1, 1)
#undef TR_STEP
  return x[0];
}
__device__ __forceinline__ void fma8(f32x2 (&acc)[4], float a, v4u t) {
#pragma unroll
  for (int k = 0; k < 4; ++k) acc[k] = __builtin_elementwise_fma(f32x2{a, a}, f32x2{bflo(t[k]), bfhi(t[k])}, acc[k]);
}
template <int I> __device__ __forceinline__ float bcast(float p) { return dpp<0x150 + I>(p); }
#define SB() __builtin_amdgcn_sched_barrier(0)
#define PIN(x) asm volatile("" : "+v"(x))

struct Args {
  const char* x; const char* k; const char* v; const char* wq; const char* wo;
  const char* wg; const char* wu; const char* wd; float* ws;
};

constexpr int L_XN = 0;
constexpr int L_QP = L_XN + 4 * 512;
constexpr int L_FP = L_QP + 4 * 128;
constexpr int L_ML = L_FP + 64;
constexpr int L_OP = L_ML + 16;
constexpr int L_END = L_OP + 4 * 128;

template <int I> struct VStep {
  template <typename F> static __device__ __forceinline__ void run(F&& f) { f.template operator()<I>(); VStep<I + 1>::run(f); }
};
template <> struct VStep<16> { template <typename F> static __device__ __forceinline__ void run(F&&) {} };

__global__ void __launch_bounds__(256, 1) k_main(Args a) {
  __shared__ __attribute__((aligned(16))) float lds[L_END];
  const int tid = threadIdx.x, lane = tid & 63, w = __builtin_amdgcn_readfirstlane(tid >> 6), r = lane >> 4, c = lane & 15;
  const int b = blockIdx.x, xg = b & 7, idx = b >> 3;
  const int head = 4 * xg + (idx >> 3), split = idx & 7;
  int fg = xg + 8 * (idx >> 2);
  const bool fvalid = fg < NFS / 4;
  if (!fvalid) fg = xg;
  const int fs = fg * 4 + (idx & 3);

  const GAS char* kp = (const GAS char*)a.k + ((size_t)head * SK + split * 4096) * (DH * 2) + w * 1024 + lane * 16;
  const GAS char* vp = (const GAS char*)a.v + ((size_t)head * SK + split * 4096) * (DH * 2) + w * 1024 + lane * 16;

  const v4u xr = ldg((const GAS char*)a.x + lane * 16);
  SB();
  v4u Kt[16], Vt[16], Wt[32];
  {
    const GAS char* qp = (const GAS char*)a.wq + (size_t)(128 * w + 32 * r) * (NH * DH * 2) + head * (DH * 2) + c * 16;
#pragma unroll
    for (int i = 0; i < 16; ++i) {
      Kt[i] = ldnt(kp + i * 4096); SB();
      Wt[2 * i] = ldg(qp + (size_t)(2 * i) * (NH * DH * 2)); SB();
      Wt[2 * i + 1] = ldg(qp + (size_t)(2 * i + 1) * (NH * DH * 2)); SB();
    }
#pragma unroll
    for (int i = 0; i < 12; ++i) { Vt[i] = ldnt(vp + i * 4096); SB(); }
  }
  {
    float xv[8];
#pragma unroll
    for (int k = 0; k < 4; ++k) { xv[2 * k] = bflo(xr[k]); xv[2 * k + 1] = bfhi(xr[k]); }
    float s = 0.f;
#pragma unroll
    for (int k = 0; k < 8; ++k) s += xv[k];
    const float mean = xrow_sum(row_sum(s)) * (1.f / DM);
    float s2 = 0.f;
#pragma unroll
    for (int k = 0; k < 8; ++k) { const float d = xv[k] - mean; s2 = fmaf(d, d, s2); }
    const float var = xrow_sum(row_sum(s2)) * (1.f / DM);
    const float mb = mrbf(mean);
    const float den = mrbf(sqrtf(mrbf(mrbf(var) + 1e-5f)));
    f32x4 o0, o1;
#pragma unroll
    for (int k = 0; k < 4; ++k) { o0[k] = mrbf(mrbf(xv[k] - mb) / den); o1[k] = mrbf(mrbf(xv[4 + k] - mb) / den); }
    *(f32x4*)(lds + L_XN + w * 512 + lane * 8) = o0;
    *(f32x4*)(lds + L_XN + w * 512 + lane * 8 + 4) = o1;
  }
  {
    f32x2 qa[4] = {};
#pragma unroll
    for (int j4 = 0; j4 < 8; ++j4) {
      const f32x4 xn = *(const f32x4*)(lds + L_XN + w * 512 + 128 * w + 32 * r + 4 * j4);
#pragma unroll
      for (int j = 0; j < 4; ++j) fma8(qa, xn[j], Wt[4 * j4 + j]);
    }
    f32x4 s0, s1;
#pragma unroll
    for (int k = 0; k < 2; ++k) { s0[2 * k] = xrow_sum(qa[k].x); s0[2 * k + 1] = xrow_sum(qa[k].y); s1[2 * k] = xrow_sum(qa[2 + k].x); s1[2 * k + 1] = xrow_sum(qa[2 + k].y); }
    if (r == 0) { *(f32x4*)(lds + L_QP + w * 128 + 8 * c) = s0; *(f32x4*)(lds + L_QP + w * 128 + 8 * c + 4) = s1; }
  }
  SB();
#pragma unroll
  for (int i = 12; i < 16; ++i) { Vt[i] = ldnt(vp + i * 4096); SB(); }
  v4u Fg[2], Fu[2];
#pragma unroll
  for (int j = 0; j < 2; ++j) {
    Fg[j] = ldg((const GAS char*)a.wg + (size_t)(tid + 256 * j) * (FF * 2) + fs * 16); SB();
    Fu[j] = ldg((const GAS char*)a.wu + (size_t)(tid + 256 * j) * (FF * 2) + fs * 16); SB();
  }
  asm volatile("s_waitcnt lgkmcnt(0)" ::: "memory");
  __builtin_amdgcn_s_barrier();
  v4u q;
  {
    f32x4 s0 = {}, s1 = {};
#pragma unroll
    for (int ww = 0; ww < 4; ++ww) { s0 += *(const f32x4*)(lds + L_QP + ww * 128 + 8 * c); s1 += *(const f32x4*)(lds + L_QP + ww * 128 + 8 * c + 4); }
    q = v4u{packbf(s0[0], s0[1]), packbf(s0[2], s0[3]), packbf(s1[0], s1[1]), packbf(s1[2], s1[3])};
  }

  constexpr float SCALE = 0.088388347648318440f, LOG2E = 1.4426950408889634f;
  float m = -INFINITY, l = 0.f;
  f32x2 acc[4] = {};
  const GAS char* wop = (const GAS char*)a.wo + (size_t)(head * DH + r) * (DM * 2) + w * 256 + c * 16;
  auto group = [&]<int MODE>(int g) {
    const GAS char* kc = kp + (size_t)g * 65536;
    const GAS char* vc = vp + (size_t)g * 65536;
    float x[16];
#pragma unroll
    for (int i = 0; i < 16; ++i) {
      PIN(Kt[i]);
      if (MODE != 0) { if (i + WIN < 16) Kt[i + WIN < 16 ? i + WIN : 0] = ldnt(kc + (i + WIN) * 4096); else Vt[(i + WIN) & 15] = ldnt(vc + ((i + WIN) & 15) * 4096); }
      if (MODE == 2) { SB(); Wt[i] = ldg(wop + (size_t)(4 * i) * (DM * 2)); }
      SB();
      float d = dot2(Kt[i][0], q[0], 0.f);
      d = dot2(Kt[i][1], q[1], d); d = dot2(Kt[i][2], q[2], d); d = dot2(Kt[i][3], q[3], d);
      x[i] = d;
      PIN(x[i]);
      SB();
    }
    float s = tr_reduce16(x, c);
    s = mrbf(mrbf(s) * SCALE) * LOG2E;
    const float mn = fmaxf(m, row_max(s));
    const float alpha = __builtin_amdgcn_exp2f(m - mn);
    const float p = __builtin_amdgcn_exp2f(s - mn);
    l = fmaf(l, alpha, p); m = mn;
#pragma unroll
    for (int k = 0; k < 4; ++k) acc[k] *= alpha;
    SB();
    VStep<0>::run([&]<int i>() {
      PIN(Vt[i]);
      if (i + WIN < 16) { if (MODE != 0) Vt[i + WIN < 16 ? i + WIN : 0] = ldnt(vc + (i + WIN) * 4096); }
      else if (MODE != 2) Kt[(i + WIN) & 15] = ldnt(kc + 65536 + ((i + WIN) & 15) * 4096);
      if (MODE == 2) { SB(); Wt[16 + i] = ldg(wop + (size_t)(4 * (16 + i)) * (DM * 2)); }
      SB();
      fma8(acc, bcast<i>(p), Vt[i]);
      PIN(acc[0]); PIN(acc[1]); PIN(acc[2]); PIN(acc[3]);
      SB();
    });
  };
  group.template operator()<0>(0);
  {
    float f[16];
    const float x0 = lds[L_XN + w * 512 + tid], x1 = lds[L_XN + w * 512 + 256 + tid];
#pragma unroll
    for (int k = 0; k < 4; ++k) {
      f[2 * k] = fmaf(x1, bflo(Fg[1][k]), x0 * bflo(Fg[0][k])); f[2 * k + 1] = fmaf(x1, bfhi(Fg[1][k]), x0 * bfhi(Fg[0][k]));
      f[8 + 2 * k] = fmaf(x1, bflo(Fu[1][k]), x0 * bflo(Fu[0][k])); f[8 + 2 * k + 1] = fmaf(x1, bfhi(Fu[1][k]), x0 * bfhi(Fu[0][k]));
    }
    const float t = xrow_sum(tr_reduce16(f, c));
    if (r == 0) lds[L_FP + c * 4 + w] = t;
  }
  SB();
  for (int g = 1; g < NGRP - 1; ++g) group.template operator()<1>(g);
  group.template operator()<2>(NGRP - 1);
  v4u Dt[2];
#pragma unroll
  for (int j = 0; j < 2; ++j) Dt[j] = ldg((const GAS char*)a.wd + (size_t)(fs * 8 + 4 * j + r) * (DM * 2) + w * 256 + c * 16);

  {
    const float mw = xrow_max(m);
    const float sc = __builtin_amdgcn_exp2f(m - mw);
    const float lw = xrow_sum(row_sum(l) * sc);
    f32x4 s0, s1;
#pragma unroll
    for (int k = 0; k < 2; ++k) { s0[2 * k] = xrow_sum(acc[k].x * sc); s0[2 * k + 1] = xrow_sum(acc[k].y * sc); s1[2 * k] = xrow_sum(acc[2 + k].x * sc); s1[2 * k + 1] = xrow_sum(acc[2 + k].y * sc); }
    if (r == 0) {
      float* o = lds + L_OP + w * 128;
#pragma unroll
      for (int k = 0; k < 4; ++k) { *(f32x2*)(o + k * 32 + 2 * c) = f32x2{s0[k], s1[k]}; }
      if (c == 0) { lds[L_ML + 2 * w] = mw; lds[L_ML + 2 * w + 1] = lw; }
    }
  }
  asm volatile("s_waitcnt lgkmcnt(0)" ::: "memory");
  __builtin_amdgcn_s_barrier();
  {
    const f32x4 ml0 = *(const f32x4*)(lds + L_ML), ml1 = *(const f32x4*)(lds + L_ML + 4);
    const float M = fmaxf(fmaxf(ml0[0], ml0[2]), fmaxf(ml1[0], ml1[2]));
    float e[4] = {__builtin_amdgcn_exp2f(ml0[0] - M), __builtin_amdgcn_exp2f(ml0[2] - M), __builtin_amdgcn_exp2f(ml1[0] - M), __builtin_amdgcn_exp2f(ml1[2] - M)};
    const float L = e[0] * ml0[1] + e[1] * ml0[3] + e[2] * ml1[1] + e[3] * ml1[3];
    const float inv = 1.f / L;
#pragma unroll
    for (int k = 0; k < 4; ++k) e[k] *= inv;
    f32x2 y[4] = {};
#pragma unroll
    for (int t4 = 0; t4 < 8; ++t4) {
      f32x4 o = {};
#pragma unroll
      for (int ww = 0; ww < 4; ++ww) o += e[ww] * *(const f32x4*)(lds + L_OP + ww * 128 + r * 32 + 4 * t4);
#pragma unroll
      for (int j = 0; j < 4; ++j) fma8(y, o[j], Wt[4 * t4 + j]);
    }
    f32x4 s0, s1;
#pragma unroll
    for (int k = 0; k < 2; ++k) { s0[2 * k] = xrow_sum(y[k].x); s0[2 * k + 1] = xrow_sum(y[k].y); s1[2 * k] = xrow_sum(y[2 + k].x); s1[2 * k + 1] = xrow_sum(y[2 + k].y); }
    const int row = head * 8 + split;
    if (r < 2) *(f32x4*)(a.ws + WS_YA + row * DM + w * 128 + 8 * c + 4 * r) = r == 0 ? s0 : s1;
    if (tid == 0) a.ws[WS_LSE + row] = M + __builtin_amdgcn_logf(L);
    f32x2 yf[4] = {};
#pragma unroll
    for (int j = 0; j < 2; ++j) {
      const f32x4 gp = *(const f32x4*)(lds + L_FP + (4 * j + r) * 4), up = *(const f32x4*)(lds + L_FP + (8 + 4 * j + r) * 4);
      const float gv = mrbf((gp[0] + gp[1]) + (gp[2] + gp[3])), uv = mrbf((up[0] + up[1]) + (up[2] + up[3]));
      const float inner = 0.7978845608028654f * (gv + 0.044715f * gv * gv * gv);
      const float ge = mrbf(0.5f * gv * (1.f + tanhf(inner)));
      fma8(yf, mrbf(ge * uv), Dt[j]);
    }
#pragma unroll
    for (int k = 0; k < 2; ++k) { s0[2 * k] = xrow_sum(yf[k].x); s0[2 * k + 1] = xrow_sum(yf[k].y); s1[2 * k] = xrow_sum(yf[2 + k].x); s1[2 * k + 1] = xrow_sum(yf[2 + k].y); }
    if (fvalid && r < 2) *(f32x4*)(a.ws + WS_YF + fs * DM + w * 128 + 8 * c + 4 * r) = r == 0 ? s0 : s1;
  }
}

__global__ void __launch_bounds__(256) k_final(const float* __restrict__ ws, const __hip_bfloat16* __restrict__ x, __hip_bfloat16* __restrict__ out) {
  __shared__ __attribute__((aligned(16))) float red[4][2][8];
  const int tid = threadIdx.x, lane = tid & 63, w = tid >> 6, rg = tid >> 1, cq = tid & 1;
  const int col = blockIdx.x * 8 + cq * 4;
  const int h = rg >> 2;
  const f32x4 l0 = *(const f32x4*)(ws + WS_LSE + h * 8), l1 = *(const f32x4*)(ws + WS_LSE + h * 8 + 4);
  const f32x4 y0 = *(const f32x4*)(ws + WS_YA + (2 * rg) * DM + col), y1 = *(const f32x4*)(ws + WS_YA + (2 * rg + 1) * DM + col);
  f32x4 f = *(const f32x4*)(ws + WS_YF + rg * DM + col);
  if (rg + 128 < NFS) f += *(const f32x4*)(ws + WS_YF + (rg + 128) * DM + col);
  const float M = fmaxf(fmaxf(fmaxf(l0[0], l0[1]), fmaxf(l0[2], l0[3])), fmaxf(fmaxf(l1[0], l1[1]), fmaxf(l1[2], l1[3])));
  float e[8], L = 0.f;
#pragma unroll
  for (int k = 0; k < 4; ++k) { e[k] = __builtin_amdgcn_exp2f(l0[k] - M); e[4 + k] = __builtin_amdgcn_exp2f(l1[k] - M); }
#pragma unroll
  for (int k = 0; k < 8; ++k) L += e[k];
  const int sp = (rg & 3) * 2;
  const float c0 = (sp == 0 ? e[0] : sp == 2 ? e[2] : sp == 4 ? e[4] : e[6]) / L, c1 = (sp == 0 ? e[1] : sp == 2 ? e[3] : sp == 4 ? e[5] : e[7]) / L;
  f32x4 av = c0 * y0 + c1 * y1;
#pragma unroll
  for (int k = 0; k < 4; ++k) {
    float t = av[k]; t += dpp<XOR2>(t); t += __shfl_xor(t, 4); t += dpp<XOR8>(t); av[k] = xrow_sum(t);
    float u = f[k]; u += dpp<XOR2>(u); u += __shfl_xor(u, 4); u += dpp<XOR8>(u); f[k] = xrow_sum(u);
  }
  if (lane < 2) { *(f32x4*)&red[w][0][cq * 4] = av; *(f32x4*)&red[w][1][cq * 4] = f; }
  __syncthreads();
  if (tid < 8) {
    const float A = (red[0][0][tid] + red[1][0][tid]) + (red[2][0][tid] + red[3][0][tid]);
    const float B = (red[0][1][tid] + red[1][1][tid]) + (red[2][1][tid] + red[3][1][tid]);
    const float xv = __bfloat162float(x[blockIdx.x * 8 + tid]);
    out[blockIdx.x * 8 + tid] = __float2bfloat16(mrbf(xv + mrbf(A)) + mrbf(B));
  }
}

extern "C" void kernel_launch(void* const* d_in, const int* in_sizes, int n_in, void* d_out, int out_size, void* d_ws, size_t ws_size,
                              hipStream_t stream) {
  Args a;
  a.x = (const char*)d_in[0]; a.k = (const char*)d_in[1]; a.v = (const char*)d_in[2]; a.wq = (const char*)d_in[3]; a.wo = (const char*)d_in[4];
  a.wg = (const char*)d_in[6]; a.wu = (const char*)d_in[7]; a.wd = (const char*)d_in[8]; a.ws = (float*)d_ws;
  k_main<<<NWG, 256, 0, stream>>>(a);
  k_final<<<64, 256, 0, stream>>>((const float*)d_ws, (const __hip_bfloat16*)d_in[0], (__hip_bfloat16*)d_out);
}
```

```cpp
#include <hip/hip_runtime.h>
#include <hip/hip_bf16.h>

#define SCHED_US 70.0
#define SCHED_T0 150
#define KP16 16
#define NMIN 18
#define NMAX 44
#define NBOOST 28
#define JBOOST 32
#define SCHED_Q ((int)(512.0 / (SCHED_US * 100.0) * 65536.0))

typedef unsigned v4u __attribute__((ext_vector_type(4)));
typedef unsigned v2u __attribute__((ext_vector_type(2)));
typedef int v4i __attribute__((ext_vector_type(4)));
typedef float f32x2 __attribute__((ext_vector_type(2)));
typedef float f32x4 __attribute__((ext_vector_type(4)));
typedef __bf16 bf16x2 __attribute__((ext_vector_type(2)));
#define GAS __attribute__((address_space(1)))
#define LAS __attribute__((address_space(3)))

constexpr int DM = 512, NH = 32, SK = 32768, DH = 128, FF = 1376;
constexpr int NWG = 256, NFS = FF / 8;
constexpr int RING = 32;
constexpr int NPC = 512;
constexpr int L_SC = RING * 4096;
constexpr int L_FL = L_SC + 16384;
constexpr int L_CT = L_FL + 256;
constexpr int L_ML = L_CT + 256;
constexpr int L_OP = L_ML + 64;
constexpr int L_QP = L_OP + 2048;
constexpr int L_Q = L_QP + 256;
constexpr int L_H = L_Q + 256;
constexpr int L_RD = L_H + 64;
constexpr int L_END = L_RD + 256;
constexpr int W_EP = 0;
constexpr int W_QG = 4096;
constexpr int W_LS = W_QG + NH * 128 * 8;
constexpr int W_FG = W_LS + NWG * 16;
constexpr int W_YA = W_FG + NFS * 512;
constexpr int W_END = W_YA + 32 * 256 * 128;

__device__ __forceinline__ v4u ldg(const GAS char* p) { return *(const GAS v4u*)p; }
__device__ __forceinline__ v4u ldw(const GAS char* p) { return __builtin_nontemporal_load((const GAS v4u*)p); }
#define QSLP 4
#define FSLP 4
#define HSTART 40
#define WSTART 112
#define WOR 1
#define WT0 2000
#define WDT 150
#define FSTART 80
#define RSTART 400
__device__ __forceinline__ float bflo(unsigned u) { return __builtin_bit_cast(float, u << 16); }
__device__ __forceinline__ float bfhi(unsigned u) { return __builtin_bit_cast(float, u & 0xffff0000u); }
__device__ __forceinline__ float rbf(float x) { return (float)(__bf16)x; }
__device__ __forceinline__ unsigned packbf(float lo, float hi) {
  bf16x2 v = {(__bf16)lo, (__bf16)hi};
  return __builtin_bit_cast(unsigned, v);
}
__device__ __forceinline__ unsigned fu(float x) { return __builtin_bit_cast(unsigned, x); }
__device__ __forceinline__ float uf(unsigned x) { return __builtin_bit_cast(float, x); }
__device__ __forceinline__ float dot2(unsigned k, unsigned q, float acc) {
  return __builtin_amdgcn_fdot2_f32_bf16(__builtin_bit_cast(bf16x2, k), __builtin_bit_cast(bf16x2, q), acc, false);
}
template <int CTRL> __device__ __forceinline__ float dpp(float x) {
  return __builtin_bit_cast(float, __builtin_amdgcn_mov_dpp(__builtin_bit_cast(int, x), CTRL, 0xf, 0xf, true));
}
template <int CTRL> __device__ __forceinline__ unsigned dppu(unsigned x) { return (unsigned)__builtin_amdgcn_mov_dpp((int)x, CTRL, 0xf, 0xf, true); }
constexpr int XOR1 = 0xB1, XOR2 = 0x4E, XOR7 = 0x141, XOR8 = 0x128, ROR4 = 0x124, BCAST = 0x150;
__device__ __forceinline__ float row_sum(float x) { x += dpp<XOR8>(x); x += dpp<XOR7>(x); x += dpp<XOR2>(x); x += dpp<XOR1>(x); return x; }
__device__ __forceinline__ float row_max(float x) { x = fmaxf(x, dpp<XOR8>(x)); x = fmaxf(x, dpp<XOR7>(x)); x = fmaxf(x, dpp<XOR2>(x)); x = fmaxf(x, dpp<XOR1>(x)); return x; }
__device__ __forceinline__ float sw32_add(float a, float b) {
  auto s = __builtin_amdgcn_permlane32_swap(fu(a), fu(b), false, false);
  return uf(s[0]) + uf(s[1]);
}
__device__ __forceinline__ float sw16_add(float a, float b) {
  auto s = __builtin_amdgcn_permlane16_swap(fu(a), fu(b), false, false);
  return uf(s[0]) + uf(s[1]);
}
__device__ __forceinline__ float xrow_sum(float x) { x = sw16_add(x, x); return sw32_add(x, x); }
__device__ __forceinline__ float xrow_max(float x) {
  auto s = __builtin_amdgcn_permlane16_swap(fu(x), fu(x), false, false);
  x = fmaxf(uf(s[0]), uf(s[1]));
  auto t = __builtin_amdgcn_permlane32_swap(fu(x), fu(x), false, false);
  return fmaxf(uf(t[0]), uf(t[1]));
}
__device__ __forceinline__ f32x2 xrow_tr8(const float (&y)[8]) {
  float z[4];
#pragma unroll
  for (int k = 0; k < 4; ++k) z[k] = sw32_add(y[k], y[k + 4]);
  return f32x2{sw16_add(z[0], z[2]), sw16_add(z[1], z[3])};
}
template <int CTRL> __device__ __forceinline__ float comb(float lo, float hi, bool sel) {
  const float t1 = lo + dpp<CTRL>(lo), t2 = hi + dpp<CTRL>(hi);
  return sel ? t2 : t1;
}
__device__ __forceinline__ void fma8(f32x2 (&acc)[4], float a, v4u t) {
#pragma unroll
  for (int k = 0; k < 4; ++k) acc[k] = __builtin_elementwise_fma(f32x2{a, a}, f32x2{bflo(t[k]), bfhi(t[k])}, acc[k]);
}
#define SB() __builtin_amdgcn_sched_barrier(0)
#define PIN(x) asm volatile("" : "+v"(x))
#define MEMB() asm volatile("" ::: "memory")
#define RFL(x) __builtin_amdgcn_readfirstlane(x)
constexpr int rev4(int i) { return ((i & 1) << 3) | ((i & 2) << 1) | ((i & 4) >> 1) | ((i & 8) >> 3); }
constexpr int AUX_SC1 = 16;
constexpr int OOB = 0x7ffffff0;
constexpr int LDS_SPIN = 1 << 18, GLB_SPIN = 1 << 15;

#define STAMP(n)
#define STAMP_OUT()

struct Args {
  const char* x; const char* k; const char* v; const char* wq; const char* wo;
  const char* wg; const char* wu; const char* wd; char* ws; char* out;
};

__device__ __forceinline__ void ln_stats(v4u xr, float& mb, float& rden) {
  float xv[8];
#pragma unroll
  for (int k = 0; k < 4; ++k) { xv[2 * k] = bflo(xr[k]); xv[2 * k + 1] = bfhi(xr[k]); }
  const float s = ((xv[0] + xv[1]) + (xv[2] + xv[3])) + ((xv[4] + xv[5]) + (xv[6] + xv[7]));
  const float mean = xrow_sum(row_sum(s)) * (1.f / DM);
  float s2 = 0.f;
#pragma unroll
  for (int k = 0; k < 8; ++k) { const float d = xv[k] - mean; s2 = fmaf(d, d, s2); }
  const float var = xrow_sum(row_sum(s2)) * (1.f / DM);
  mb = rbf(mean);
  rden = __builtin_amdgcn_rcpf(rbf(__builtin_amdgcn_sqrtf(rbf(rbf(var) + 1e-5f))));
}
__device__ __forceinline__ float ln_elem(float x, float mb, float rden) { return rbf(rbf(x - mb) * rden); }

template <int SLP> __device__ __forceinline__ void wait_lds(const volatile LAS int* p, int need, int& known) {
  if (known < need) {
#pragma nounroll
    for (int spins = 0;; ++spins) {
      known = RFL(*p);
      if (known >= need) break;
      if (spins > LDS_SPIN) { known = 0x7fffffff; break; }
      __builtin_amdgcn_s_sleep(SLP);
    }
  }
  MEMB();
}
__device__ __forceinline__ void wait_until(unsigned t) {
#pragma nounroll
  for (int spins = 0; spins < 4096 && (int)((unsigned)__builtin_amdgcn_s_memrealtime() - t) < 0; ++spins) __builtin_amdgcn_s_sleep(2);
  MEMB();
}
template <int SLP> __device__ __forceinline__ void wait_flags4(const volatile LAS int* p) {
#pragma nounroll
  for (int spins = 0; spins <= LDS_SPIN; ++spins) {
    const v4i d = *(const volatile LAS v4i*)p;
    if (RFL(d[0] & d[1] & d[2] & d[3])) break;
    __builtin_amdgcn_s_sleep(SLP);
  }
  MEMB();
}
template <int N, int SLP> __device__ __forceinline__ void sweep(__amdgpu_buffer_rsrc_t rs, int (&off)[N], unsigned tag, f32x2 (&val)[N]) {
#pragma nounroll
  for (int spins = 0;; ++spins) {
    MEMB();
    v4u g[N];
#pragma unroll
    for (int i = 0; i < N; ++i) g[i] = __builtin_amdgcn_raw_buffer_load_b128(rs, off[i], 0, AUX_SC1);
    int left = OOB;
#pragma unroll
    for (int i = 0; i < N; ++i) {
      const bool ok = off[i] != OOB && g[i][1] == tag && g[i][3] == tag;
      val[i].x = ok ? uf(g[i][0]) : val[i].x; val[i].y = ok ? uf(g[i][2]) : val[i].y;
      off[i] = ok ? OOB : off[i];
      left &= off[i];
    }
    if (__all(left == OOB) || spins > GLB_SPIN) break;
    __builtin_amdgcn_s_sleep(SLP);
  }
  MEMB();
}

#define BLK(n) "s_waitcnt vmcnt(" #n ")\n\tglobal_load_lds_dwordx4 %0, %1 nt\n\t" \
               "s_waitcnt vmcnt(" #n ")\n\tglobal_load_lds_dwordx4 %0, %1 offset:1024 nt\n\t" \
               "s_waitcnt vmcnt(" #n ")\n\tglobal_load_lds_dwordx4 %0, %1 offset:2048 nt\n\t" \
               "s_waitcnt vmcnt(" #n ")\n\tglobal_load_lds_dwordx4 %0, %1 offset:3072 nt\n\t" \
               "s_branch 9f\n\t"
__device__ __forceinline__ void issue_piece(unsigned voff, const char* p, unsigned dst, unsigned sel, unsigned faddr, int pub) {
  asm volatile("s_mov_b32 m0, %2\n\t"
               "s_getpc_b64 vcc\n\t"
               "s_add_u32 vcc_lo, vcc_lo, %3\n\t"
               "s_addc_u32 vcc_hi, vcc_hi, 0\n\t"
               "s_setpc_b64 vcc\n\t"
               BLK(15) BLK(16) BLK(17) BLK(18) BLK(19) BLK(20) BLK(21) BLK(22) BLK(23) BLK(24) BLK(25) BLK(26) BLK(27) BLK(28) BLK(29)
               BLK(30) BLK(31) BLK(32) BLK(33) BLK(34) BLK(35) BLK(36) BLK(37) BLK(38) BLK(39) BLK(40) BLK(41) BLK(42) BLK(43)
               "9:\n\t"
               "ds_write_b32 %4, %5"
               :: "v"(voff), "s"(p), "s"(dst), "s"(sel), "v"(faddr), "v"(pub) : "memory", "scc", "vcc");
}
#undef BLK

template <int I> struct Unroll {
  template <typename F> static __device__ __forceinline__ void run(F&& f) { Unroll<I - 1>::run(f); f.template operator()<I - 1>(); }
};
template <> struct Unroll<0> { template <typename F> static __device__ __forceinline__ void run(F&&) {} };

__global__ void __launch_bounds__(576, 1) k_main(Args a) {
  __shared__ __attribute__((aligned(16))) char smem[L_END];
  LAS char* L = (LAS char*)smem;
  const int tid = threadIdx.x, lane = tid & 63, w = RFL(tid >> 6), r = lane >> 4, c = lane & 15;
  const int b = blockIdx.x, xg = b & 7, idx = b >> 3;
  const int head = 4 * xg + (idx >> 3), split = idx & 7;
  volatile LAS int* landed = (volatile LAS int*)(L + L_FL);
  volatile LAS int* ctl = (volatile LAS int*)(L + L_CT);
  STAMP(0);
  if (w == 0) { ctl[lane] = 0; landed[lane] = 0; }
  asm volatile("s_waitcnt lgkmcnt(0)" ::: "memory");
  __builtin_amdgcn_s_barrier();
  SB();

  if (__builtin_expect(w == 0, 1)) {
    __builtin_amdgcn_s_setprio(3);
    const unsigned t0 = (unsigned)__builtin_amdgcn_s_memrealtime();
    STAMP(12);
    const char* p = a.k + (size_t)head * (SK * DH * 2) + split * 8192;
    const long jump = a.v - a.k;
    unsigned pos = head * 4;
    const unsigned voff = lane * 16;
    const unsigned lbase = (unsigned)(size_t)L;
    const unsigned faddr = lbase + L_FL + lane * 4;
    int pub = 0, limit = RING;
    unsigned tn = t0;
#pragma unroll 1
    for (int j = 0; j < NPC; j += 4) {
      const int lag16 = (int)(((long)(int)(tn - t0 - SCHED_T0) * SCHED_Q) >> 12) - 16 * (j - 4);
      int n = min(max(NMIN + ((lag16 * KP16) >> 8), NMIN), NMAX);
      if (j < JBOOST) n = max(n, NBOOST);
      const unsigned sel = (n - 16) * 52 + 12;
      const int lagp = (n + 3) >> 2;
      if (j >= limit) {
#pragma nounroll
        for (int spins = 0;; ++spins) {
          const v4i d = *(const volatile LAS v4i*)ctl;
          limit = RFL(min(min(d[0], d[1]), min(d[2], d[3]))) * 4 + RING;
          if (j < limit) break;
          if (spins > LDS_SPIN) { limit = 0x7fffffff; break; }
          __builtin_amdgcn_s_sleep(1);
        }
      }
      unsigned long long tq;
      asm volatile("s_memrealtime %0" : "=s"(tq) :: "memory");
#pragma unroll 1
      for (int k = 0; k < 4; ++k) {
        pub = max(pub, j + k + 1 - lagp);
        issue_piece(voff, p + ((pos + (k >> 1)) & 127) * 65536 + (k & 1) * 4096, lbase + ((j + k) & (RING - 1)) * 4096, sel, faddr, pub);
      }
      pos += 2;
      if (j == NPC / 2 - 4) p += jump;
      asm volatile("s_waitcnt lgkmcnt(0)" : "+s"(tq) :: "memory");
      tn = (unsigned)tq;
    }
    STAMP(1);
    Unroll<NMAX / 4 + 1>::run([&]<int i>() {
      constexpr int left = NMAX / 4 - i;
      asm volatile("s_waitcnt vmcnt(%c0)" :: "i"(4 * left) : "memory");
      if (NPC - left > pub) landed[lane] = NPC - left;
    });
    STAMP(2);
    STAMP_OUT();
    return;
  }

  const __amdgpu_buffer_rsrc_t rs = __builtin_amdgcn_make_buffer_rsrc(a.ws, 0, W_END, 0x00020000);
  const unsigned tag = *(const __attribute__((address_space(4))) unsigned*)(a.ws + W_EP) + 1u;
  v4u xr = ldg((const GAS char*)a.x + lane * 16);

  if (w <= 4) {
    const int cw = w - 1;
    v4u q = {};
    {
      const int half = lane >> 5, rr = lane & 31;
      const v2u x4 = *(const GAS v2u*)((const GAS char*)a.x + (128 * cw + 4 * rr) * 2);
      const GAS char* qp = (const GAS char*)a.wq + (size_t)(128 * cw + 4 * rr) * (NH * DH * 2) + (head * DH + 16 * split + 8 * half) * 2;
      v4u wt[4];
#pragma unroll
      for (int i = 0; i < 4; ++i) wt[i] = ldw(qp + (size_t)i * (NH * DH * 2));
      SB();
      PIN(xr);
      float mb, rden;
      ln_stats(xr, mb, rden);
      f32x2 qa[4] = {};
      fma8(qa, ln_elem(bflo(x4[0]), mb, rden), wt[0]);
      fma8(qa, ln_elem(bfhi(x4[0]), mb, rden), wt[1]);
      fma8(qa, ln_elem(bflo(x4[1]), mb, rden), wt[2]);
      fma8(qa, ln_elem(bfhi(x4[1]), mb, rden), wt[3]);
      const float u0 = comb<XOR8>(qa[0].x, qa[2].x, c & 8), u1 = comb<XOR8>(qa[0].y, qa[2].y, c & 8);
      const float u2 = comb<XOR8>(qa[1].x, qa[3].x, c & 8), u3 = comb<XOR8>(qa[1].y, qa[3].y, c & 8);
      const float v0 = comb<XOR7>(u0, u2, c & 4), v1 = comb<XOR7>(u1, u3, c & 4);
      float t = comb<XOR2>(v0, v1, c & 2);
      t += dpp<XOR1>(t);
      t = sw16_add(t, t);
      if ((lane & 17) == 0) *(volatile LAS float*)(L + L_QP + (8 * half + (c >> 1)) * 16 + cw * 4) = t;
      asm volatile("s_waitcnt lgkmcnt(0)" ::: "memory");
      ctl[8 + cw] = 1;
      STAMP(1);
    }
    constexpr float SCALE = 0.088388347648318440f, LOG2E = 1.4426950408889634f;
    LAS float* sc = (LAS float*)(L + L_SC) + cw * 1024 + lane;
    int known = 0;
    auto fetch = [&](int n, v4u (&t)[4]) {
      const int j = 4 * n + cw;
      wait_lds<1>(landed, j + 1, known);
      const LAS v4u* src = (const LAS v4u*)(L + (j & (RING - 1)) * 4096) + lane;
#pragma unroll
      for (int k = 0; k < 4; ++k) t[k] = src[64 * k];
      asm volatile("s_waitcnt lgkmcnt(0)" : "+v"(t[0]), "+v"(t[1]), "+v"(t[2]), "+v"(t[3]) :: "memory");
      ctl[cw] = n + 1;
      MEMB();
    };
    float m = -INFINITY;
#pragma unroll 1
    for (int g = 0; g < 16; ++g) {
      v4u t[4][4];
#pragma unroll
      for (int pi = 0; pi < 4; ++pi) fetch(4 * g + pi, t[pi]);
      if (g == 0) {
        int kq = 0;
        wait_lds<1>(ctl + 16, 1, kq);
        q = *(const LAS v4u*)(L + L_Q + c * 16);
        STAMP(2);
      }
      float z[4];
#pragma unroll
      for (int pi = 0; pi < 4; ++pi) {
        float x[4];
#pragma unroll
        for (int k = 0; k < 4; ++k) x[k] = dot2(t[pi][k][3], q[3], dot2(t[pi][k][2], q[2], dot2(t[pi][k][1], q[1], dot2(t[pi][k][0], q[0], 0.f))));
        z[pi] = comb<XOR7>(comb<XOR8>(x[0], x[1], c & 8), comb<XOR8>(x[2], x[3], c & 8), c & 4);
      }
      float s = comb<XOR1>(comb<XOR2>(z[0], z[1], c & 2), comb<XOR2>(z[2], z[3], c & 2), c & 1);
      s = rbf(rbf(s) * SCALE) * LOG2E;
      m = fmaxf(m, s);
      sc[g * 64] = s;
    }
    STAMP(3);
    m = xrow_max(row_max(m));
    {
      float l = 0.f;
#pragma unroll
      for (int g = 0; g < 16; ++g) { const float p = __builtin_amdgcn_exp2f(sc[g * 64] - m); l += p; sc[g * 64] = p; }
      l = xrow_sum(row_sum(l));
      *(volatile LAS f32x2*)(L + L_ML + cw * 8) = f32x2{m, l};
      asm volatile("s_waitcnt lgkmcnt(0)" ::: "memory");
      ctl[4 + cw] = 1;
    }
    STAMP(4);
    f32x2 acc[4] = {};
#pragma unroll 1
    for (int g = 0; g < 16; ++g) {
      const float pv = sc[g * 64];
      Unroll<4>::run([&]<int pi>() {
        v4u t[4];
        fetch(64 + 4 * g + pi, t);
        Unroll<4>::run([&]<int k>() { fma8(acc, dpp<BCAST + rev4(4 * pi + k)>(pv), t[k]); });
        PIN(acc[0]); PIN(acc[1]); PIN(acc[2]); PIN(acc[3]);
      });
    }
    STAMP(5);
    {
      const float y[8] = {acc[0].x, acc[0].y, acc[1].x, acc[1].y, acc[2].x, acc[2].y, acc[3].x, acc[3].y};
      *(LAS f32x2*)(L + L_OP + cw * 512 + lane * 8) = xrow_tr8(y);
    }
    asm volatile("s_waitcnt lgkmcnt(0)" ::: "memory");
    __builtin_amdgcn_s_barrier();
    STAMP(6);
    STAMP_OUT();
    return;
  }

  const int hw = w - 5;
  const unsigned tstart = (unsigned)__builtin_amdgcn_s_memrealtime();
  if (hw == 0) {
    wait_flags4<1>(ctl + 8);
    const f32x4 pp = *(const LAS f32x4*)(L + L_QP + c * 16);
    if (lane < 16) __builtin_amdgcn_raw_buffer_store_b64(v2u{fu((pp[0] + pp[1]) + (pp[2] + pp[3])), tag}, rs, W_QG + (head * 128 + 16 * split + lane) * 8, 0, AUX_SC1);
    STAMP(10);
    int off[1] = {W_QG + head * 1024 + lane * 16};
    f32x2 val[1] = {};
    sweep<1, QSLP>(rs, off, tag, val);
    *(volatile LAS unsigned*)(L + L_Q + lane * 4) = packbf(val[0].x, val[0].y);
    asm volatile("s_waitcnt lgkmcnt(0)" ::: "memory");
    ctl[16] = 1;
    STAMP(11);
  }
  int known = 0;
  wait_lds<8>(landed, HSTART, known);
  STAMP(1);
  if (hw == 1 && b < NFS) {
    const int mh = b / 43, li = b - 43 * mh, pr = lane >> 3, ch = lane & 7;
    const int m0 = 128 * (mh & 1) + 16 * pr;
    const GAS char* src = (const GAS char*)(mh < 2 ? a.wg : a.wu) + m0 * (4 * FF) + li * 128 + ch * 16;
    const bool odd = li * 8 + ch >= NFS;
    v4u xq[4], t[16];
#pragma unroll
    for (int i = 0; i < 4; ++i) xq[i] = ldg((const GAS char*)a.x + m0 * 4 + i * 16);
#pragma unroll
    for (int i = 0; i < 16; ++i) t[i] = ldw(src + i * (4 * FF));
    SB();
    float mb, rden;
    ln_stats(xr, mb, rden);
    f32x2 ga[4] = {};
#pragma unroll
    for (int i = 0; i < 16; ++i) fma8(ga, ln_elem(odd ? bfhi(xq[i >> 2][i & 3]) : bflo(xq[i >> 2][i & 3]), mb, rden), t[i]);
    const float yg[8] = {ga[0].x, ga[0].y, ga[1].x, ga[1].y, ga[2].x, ga[2].y, ga[3].x, ga[3].y};
    f32x2 g2 = xrow_tr8(yg);
    g2.x += dpp<XOR8>(g2.x); g2.y += dpp<XOR8>(g2.y);
    if (c < 8) __builtin_amdgcn_raw_buffer_store_b128(v4u{fu(g2.x), tag, fu(g2.y), tag}, rs, W_FG + (b * 4 + r) * 128 + c * 16, 0, AUX_SC1);
  }
  STAMP(12);
  f32x2 yf2 = {};
  if (b >= NWG - NFS) {
    const int fs = b - (NWG - NFS);
    wait_lds<16>(landed, FSTART, known);
    const GAS char* dp = (const GAS char*)a.wd + (size_t)(fs * 8 + r) * (DM * 2) + hw * 256 + c * 16;
    const v4u d0 = ldw(dp), d1 = ldw(dp + 4 * (DM * 2));
    if (hw == 1) {
      const int cidx = fs + ((lane >> 2) & 1) * NFS;
      int off[1] = {lane < 32 ? W_FG + (((lane >> 3) * 43 + (cidx >> 3)) * 4 + (lane & 3)) * 128 + (cidx & 7) * 16 : OOB};
      f32x2 val[1] = {};
      sweep<1, 32>(rs, off, tag, val);
      float e[2] = {val[0].x, val[0].y}, hv[2];
#pragma unroll
      for (int k = 0; k < 2; ++k) {
        e[k] += dpp<ROR4>(e[k]); e[k] += dpp<XOR8>(e[k]);
        auto sw = __builtin_amdgcn_permlane16_swap(fu(e[k]), fu(e[k]), false, false);
        const float gv = rbf(uf(sw[0])), uv = rbf(uf(sw[1]));
        const float inner = 0.7978845608028654f * (gv + 0.044715f * gv * gv * gv);
        const float ge = rbf(gv * __builtin_amdgcn_rcpf(1.f + __builtin_amdgcn_exp2f(-2.885390081777927f * inner)));
        hv[k] = rbf(ge * uv);
      }
      if (lane < 4) *(volatile LAS f32x2*)(L + L_H + lane * 8) = f32x2{hv[0], hv[1]};
      asm volatile("s_waitcnt lgkmcnt(0)" ::: "memory");
      ctl[12] = 1;
    }
    int kh = 0;
    wait_lds<8>(ctl + 12, 1, kh);
    f32x2 yf[4] = {};
    fma8(yf, *(const LAS float*)(L + L_H + r * 4), d0);
    fma8(yf, *(const LAS float*)(L + L_H + 16 + r * 4), d1);
    const float y[8] = {yf[0].x, yf[0].y, yf[1].x, yf[1].y, yf[2].x, yf[2].y, yf[3].x, yf[3].y};
    yf2 = xrow_tr8(y);
  }
  PIN(yf2);
  STAMP(2);
  unsigned wp[16][8];
  {
    const GAS char* wop = (const GAS char*)a.wo + (size_t)(head * DH + 2 * r) * (DM * 2) + hw * 256 + c * 16;
#pragma unroll
    for (int u0 = 0; u0 < 16; u0 += WOR) {
      wait_until(tstart + WT0 + (u0 / WOR) * WDT);
      v4u A[WOR], B[WOR];
#pragma unroll
      for (int i = 0; i < WOR; ++i) { A[i] = ldw(wop + (8 * (u0 + i)) * (DM * 2)); B[i] = ldw(wop + (8 * (u0 + i) + 1) * (DM * 2)); }
      SB();
#pragma unroll
      for (int i = 0; i < WOR; ++i) {
#pragma unroll
        for (int k = 0; k < 4; ++k) { wp[u0 + i][2 * k] = __builtin_amdgcn_perm(B[i][k], A[i][k], 0x05040100u); wp[u0 + i][2 * k + 1] = __builtin_amdgcn_perm(B[i][k], A[i][k], 0x07060302u); }
#pragma unroll
        for (int k = 0; k < 8; ++k) PIN(wp[u0 + i][k]);
      }
      SB();
    }
  }
  STAMP(3);
  float es[4];
  {
    wait_flags4<32>(ctl + 4);
    const f32x4 ml0 = *(const LAS f32x4*)(L + L_ML), ml1 = *(const LAS f32x4*)(L + L_ML + 16);
    const float M = fmaxf(fmaxf(ml0[0], ml0[2]), fmaxf(ml1[0], ml1[2]));
    const float Ls = (__builtin_amdgcn_exp2f(ml0[0] - M) * ml0[1] + __builtin_amdgcn_exp2f(ml0[2] - M) * ml0[3]) +
                     (__builtin_amdgcn_exp2f(ml1[0] - M) * ml1[1] + __builtin_amdgcn_exp2f(ml1[2] - M) * ml1[3]);
    if (hw == 0 && lane == 0) __builtin_amdgcn_raw_buffer_store_b128(v4u{fu(M), tag, fu(Ls), tag}, rs, W_LS + (head * 8 + split) * 16, 0, AUX_SC1);
    STAMP(4);
    wait_lds<32>(landed, RSTART, known);
    int off[1] = {W_LS + (head * 8 + (lane & 7)) * 16};
    f32x2 val[1] = {};
    sweep<1, 32>(rs, off, tag, val);
    float MH = fmaxf(val[0].x, dpp<XOR7>(val[0].x)); MH = fmaxf(MH, dpp<XOR2>(MH)); MH = fmaxf(MH, dpp<XOR1>(MH));
    float LH = __builtin_amdgcn_exp2f(val[0].x - MH) * val[0].y;
    LH += dpp<XOR7>(LH); LH += dpp<XOR2>(LH); LH += dpp<XOR1>(LH);
    const float inv = __builtin_amdgcn_rcpf(LH);
    es[0] = uf(RFL(fu(__builtin_amdgcn_exp2f(ml0[0] - MH) * inv))); es[1] = uf(RFL(fu(__builtin_amdgcn_exp2f(ml0[2] - MH) * inv)));
    es[2] = uf(RFL(fu(__builtin_amdgcn_exp2f(ml1[0] - MH) * inv))); es[3] = uf(RFL(fu(__builtin_amdgcn_exp2f(ml1[2] - MH) * inv)));
  }
  const bool reducer = (idx & 7) == 2;
  const int rid = xg * 4 + (idx >> 3);
  const int ocol = 128 * (rid >> 3) + 64 * (rid & 1) + 8 * (lane & 7) + 2 * ((rid >> 1) & 3);
  unsigned x2 = *(const GAS unsigned*)((const GAS char*)a.x + ocol * 2);
  PIN(x2);
  STAMP(5);
  SB();
  __builtin_amdgcn_s_barrier();
  SB();
  STAMP(6);
  {
    f32x2 o = {};
#pragma unroll
    for (int k = 0; k < 4; ++k) o = __builtin_elementwise_fma(f32x2{es[k], es[k]}, *(const LAS f32x2*)(L + L_OP + k * 512 + lane * 8), o);
    const unsigned op = packbf(o.x, o.y);
    float y[8] = {};
    Unroll<16>::run([&]<int u>() {
      const unsigned ob = dppu<BCAST + u>(op);
#pragma unroll
      for (int k = 0; k < 8; ++k) y[k] = dot2(ob, wp[u][k], y[k]);
    });
    const f32x2 yy = xrow_tr8(y) + yf2;
    __builtin_amdgcn_raw_buffer_store_b128(v4u{fu(yy.x), tag, fu(yy.y), tag}, rs, W_YA + ((8 * hw + (lane >> 3)) * 256 + b) * 128 + (lane & 7) * 16, 0, AUX_SC1);
  }
  STAMP(7);
  if (reducer) {
    int off[8];
    f32x2 val[8] = {};
#pragma unroll
    for (int i = 0; i < 8; ++i) off[i] = W_YA + (rid * 256 + 64 * hw + 8 * i) * 128 + lane * 16;
    sweep<8, FSLP>(rs, off, tag, val);
    STAMP(8);
    const f32x2 A = ((val[0] + val[1]) + (val[2] + val[3])) + ((val[4] + val[5]) + (val[6] + val[7]));
    const float As = xrow_sum(comb<XOR8>(A.x, A.y, c & 8));
    if (lane < 16) *(volatile LAS float*)(L + L_RD + lane * 16 + hw * 4) = As;
    asm volatile("s_waitcnt lgkmcnt(0)" ::: "memory");
    __builtin_amdgcn_s_barrier();
    if (hw == 0) {
      const f32x4 pa = *(const LAS f32x4*)(L + L_RD + c * 16);
      const float o = ((c & 8) ? bfhi(x2) : bflo(x2)) + ((pa[0] + pa[1]) + (pa[2] + pa[3]));
      const float o1 = dpp<XOR8>(o);
      if (lane < 8) *(GAS unsigned*)((GAS char*)a.out + ocol * 2) = packbf(o, o1);
      if (rid == 0 && lane == 0) __builtin_amdgcn_raw_buffer_store_b32(tag, rs, W_EP, 0, AUX_SC1);
    }
  }
  STAMP(9);
  STAMP_OUT();
}

extern "C" void kernel_launch(void* const* d_in, const int* in_sizes, int n_in, void* d_out, int out_size, void* d_ws, size_t ws_size,
                              hipStream_t stream) {
  Args a;
  a.x = (const char*)d_in[0]; a.k = (const char*)d_in[1]; a.v = (const char*)d_in[2]; a.wq = (const char*)d_in[3]; a.wo = (const char*)d_in[4];
  a.wg = (const char*)d_in[6]; a.wu = (const char*)d_in[7]; a.wd = (const char*)d_in[8]; a.ws = (char*)d_ws; a.out = (char*)d_out;
  k_main<<<NWG, 576, 0, stream>>>(a);
}
```

```cpp
#include <hip/hip_runtime.h>
#include <hip/hip_bf16.h>

typedef unsigned v4u __attribute__((ext_vector_type(4)));
typedef unsigned v2u __attribute__((ext_vector_type(2)));
typedef int v4i __attribute__((ext_vector_type(4)));
typedef float f32x2 __attribute__((ext_vector_type(2)));
typedef float f32x4 __attribute__((ext_vector_type(4)));
typedef __bf16 bf16x2 __attribute__((ext_vector_type(2)));
#define GAS __attribute__((address_space(1)))
#define LAS __attribute__((address_space(3)))

constexpr int DM = 512, NH = 32, SK = 32768, DH = 128, FF = 1376;
constexpr int NWG = 256, NFS = FF / 8;
constexpr int RING = 32;
constexpr int NPC = 512;
constexpr double SCHED_US = 70.0;
constexpr int KP16 = 24, N0_16 = 166;
constexpr int NMIN = 8, NMAX = 24, NBOOST = 12, JBOOST = 8, I0 = 4;
constexpr int PC_FIRST = 24, PC_STEP = 16, PC_DLY = 8, PC_N = 13;
constexpr int VCAP = 60;
constexpr int SCHED_Q = (int)(256.0 / (SCHED_US * 100.0) * 65536.0);
constexpr int TAU_Q = (int)(SCHED_US * 100.0);
constexpr int CHB = 14;
constexpr int HSTART = 20, FSTART = 48, RSTART = 200, WT0 = 2800, WDT = 150, WLEAD = 55;
constexpr int L_SC = RING * 4096;
constexpr int L_FL = L_SC + 16384;
constexpr int L_CT = L_FL + 512;
constexpr int L_ML = L_CT + 256;
constexpr int L_OP = L_ML + 64;
constexpr int L_QP = L_OP + 2048;
constexpr int L_Q = L_QP + 4608;
constexpr int L_H = L_Q + 512;
constexpr int L_RD = L_H + 64;
constexpr int L_END = L_RD + 256;
constexpr int W_EP = 0;
constexpr int W_PC = 1024;
static_assert(W_PC + PC_N * 128 <= 4096, "");
constexpr int W_QG = 4096;
constexpr int W_LS = W_QG + NWG * 512;
constexpr int W_FG = W_LS + NWG * 16;
constexpr int W_YA = W_FG + NFS * 512;
constexpr int W_END = W_YA + 32 * 256 * 128;

__device__ __forceinline__ v4u ldg(const GAS char* p) { return *(const GAS v4u*)p; }
__device__ __forceinline__ v4u ldw(const GAS char* p) { return __builtin_nontemporal_load((const GAS v4u*)p); }
__device__ __forceinline__ float bflo(unsigned u) { return __builtin_bit_cast(float, u << 16); }
__device__ __forceinline__ float bfhi(unsigned u) { return __builtin_bit_cast(float, u & 0xffff0000u); }
__device__ __forceinline__ float rbf(float x) { return (float)(__bf16)x; }
__device__ __forceinline__ unsigned packbf(float lo, float hi) {
  bf16x2 v = {(__bf16)lo, (__bf16)hi};
  return __builtin_bit_cast(unsigned, v);
}
__device__ __forceinline__ unsigned fu(float x) { return __builtin_bit_cast(unsigned, x); }
__device__ __forceinline__ float uf(unsigned x) { return __builtin_bit_cast(float, x); }
__device__ __forceinline__ float dot2(unsigned k, unsigned q, float acc) {
  return __builtin_amdgcn_fdot2_f32_bf16(__builtin_bit_cast(bf16x2, k), __builtin_bit_cast(bf16x2, q), acc, false);
}
template <int CTRL> __device__ __forceinline__ float dpp(float x) {
  return __builtin_bit_cast(float, __builtin_amdgcn_mov_dpp(__builtin_bit_cast(int, x), CTRL, 0xf, 0xf, true));
}
template <int CTRL> __device__ __forceinline__ unsigned dppu(unsigned x) { return (unsigned)__builtin_amdgcn_mov_dpp((int)x, CTRL, 0xf, 0xf, true); }
constexpr int XOR1 = 0xB1, XOR2 = 0x4E, XOR7 = 0x141, XOR8 = 0x128, ROR4 = 0x124, BCAST = 0x150;
__device__ __forceinline__ float row_sum(float x) { x += dpp<XOR8>(x); x += dpp<XOR7>(x); x += dpp<XOR2>(x); x += dpp<XOR1>(x); return x; }
__device__ __forceinline__ float row_max(float x) { x = fmaxf(x, dpp<XOR8>(x)); x = fmaxf(x, dpp<XOR7>(x)); x = fmaxf(x, dpp<XOR2>(x)); x = fmaxf(x, dpp<XOR1>(x)); return x; }
__device__ __forceinline__ float sw32_add(float a, float b) {
  auto s = __builtin_amdgcn_permlane32_swap(fu(a), fu(b), false, false);
  return uf(s[0]) + uf(s[1]);
}
__device__ __forceinline__ float sw16_add(float a, float b) {
  auto s = __builtin_amdgcn_permlane16_swap(fu(a), fu(b), false, false);
  return uf(s[0]) + uf(s[1]);
}
__device__ __forceinline__ float xrow_sum(float x) { x = sw16_add(x, x); return sw32_add(x, x); }
__device__ __forceinline__ float xrow_max(float x) {
  auto s = __builtin_amdgcn_permlane16_swap(fu(x), fu(x), false, false);
  x = fmaxf(uf(s[0]), uf(s[1]));
  auto t = __builtin_amdgcn_permlane32_swap(fu(x), fu(x), false, false);
  return fmaxf(uf(t[0]), uf(t[1]));
}
__device__ __forceinline__ f32x2 xrow_tr8(const float (&y)[8]) {
  float z[4];
#pragma unroll
  for (int k = 0; k < 4; ++k) z[k] = sw32_add(y[k], y[k + 4]);
  return f32x2{sw16_add(z[0], z[2]), sw16_add(z[1], z[3])};
}
template <int CTRL> __device__ __forceinline__ float comb(float lo, float hi, bool sel) {
  const float t1 = lo + dpp<CTRL>(lo), t2 = hi + dpp<CTRL>(hi);
  return sel ? t2 : t1;
}
__device__ __forceinline__ void fma8(f32x2 (&acc)[4], float a, v4u t) {
#pragma unroll
  for (int k = 0; k < 4; ++k) acc[k] = __builtin_elementwise_fma(f32x2{a, a}, f32x2{bflo(t[k]), bfhi(t[k])}, acc[k]);
}
#define SB() __builtin_amdgcn_sched_barrier(0)
#define PIN(x) asm volatile("" : "+v"(x))
#define MEMB() asm volatile("" ::: "memory")
#define BARRIER() asm volatile("s_waitcnt lgkmcnt(0)\n\ts_barrier" ::: "memory")
#define RFL(x) __builtin_amdgcn_readfirstlane(x)
constexpr int rev4(int i) { return ((i & 1) << 3) | ((i & 2) << 1) | ((i & 4) >> 1) | ((i & 8) >> 3); }
constexpr int AUX_SC1 = 16;
constexpr int OOB = 0x7ffffff0;
constexpr int LDS_SPIN = 1 << 22, GLB_SPIN = 1 << 18;


struct Args {
  const char* x; const char* k; const char* v; const char* wq; char* ws;
  const char* wo; const char* wg; const char* wu; const char* wd; char* out;
};

__device__ __forceinline__ void ln_stats(v4u xr, float& mb, float& rden) {
  float xv[8];
#pragma unroll
  for (int k = 0; k < 4; ++k) { xv[2 * k] = bflo(xr[k]); xv[2 * k + 1] = bfhi(xr[k]); }
  const float s = ((xv[0] + xv[1]) + (xv[2] + xv[3])) + ((xv[4] + xv[5]) + (xv[6] + xv[7]));
  const float mean = xrow_sum(row_sum(s)) * (1.f / DM);
  float s2 = 0.f;
#pragma unroll
  for (int k = 0; k < 8; ++k) { const float d = xv[k] - mean; s2 = fmaf(d, d, s2); }
  const float var = xrow_sum(row_sum(s2)) * (1.f / DM);
  mb = rbf(mean);
  rden = __builtin_amdgcn_rcpf(rbf(__builtin_amdgcn_sqrtf(rbf(rbf(var) + 1e-5f))));
}
__device__ __forceinline__ float ln_elem(float x, float mb, float rden) { return rbf(rbf(x - mb) * rden); }

template <int SLP> __device__ __forceinline__ void wait_lds(const volatile LAS int* p, int need, int& known) {
  if (known < need) {
#pragma nounroll
    for (int spins = 0;; ++spins) {
      known = RFL(*p);
      if (known >= need) break;
      if (spins > LDS_SPIN) { known = 0x7fffffff; break; }
      __builtin_amdgcn_s_sleep(SLP);
    }
  }
  MEMB();
}
__device__ __forceinline__ void wait_until(unsigned t) {
#pragma nounroll
  for (int spins = 0; spins < 4096 && (int)((unsigned)__builtin_amdgcn_s_memrealtime() - t) < 0; ++spins) __builtin_amdgcn_s_sleep(2);
  MEMB();
}
template <int SLP> __device__ __forceinline__ void wait_flags4(const volatile LAS int* p) {
#pragma nounroll
  for (int spins = 0; spins <= LDS_SPIN; ++spins) {
    const v4i d = *(const volatile LAS v4i*)p;
    if (RFL(d[0] & d[1] & d[2] & d[3])) break;
    __builtin_amdgcn_s_sleep(SLP);
  }
  MEMB();
}
template <int N, int SLP> __device__ __forceinline__ void sweep(__amdgpu_buffer_rsrc_t rs, int (&off)[N], unsigned tag, f32x2 (&val)[N]) {
#pragma nounroll
  for (int spins = 0;; ++spins) {
    MEMB();
    v4u g[N];
#pragma unroll
    for (int i = 0; i < N; ++i) g[i] = __builtin_amdgcn_raw_buffer_load_b128(rs, off[i], 0, AUX_SC1);
    int left = OOB;
#pragma unroll
    for (int i = 0; i < N; ++i) {
      const bool ok = off[i] != OOB && g[i][1] == tag && g[i][3] == tag;
      val[i].x = ok ? uf(g[i][0]) : val[i].x; val[i].y = ok ? uf(g[i][2]) : val[i].y;
      off[i] = ok ? OOB : off[i];
      left &= off[i];
    }
    if (__all(left == OOB) || spins > GLB_SPIN) break;
    if (SLP) __builtin_amdgcn_s_sleep(SLP);
  }
  MEMB();
}

template <int I> struct Unroll {
  template <typename F> static __device__ __forceinline__ void run(F&& f) { Unroll<I - 1>::run(f); f.template operator()<I - 1>(); }
};
template <> struct Unroll<0> { template <typename F> static __device__ __forceinline__ void run(F&&) {} };

static_assert(RING == 32 && NPC == 512 && NMIN >= 8 && NMAX == 24 && NBOOST >= 8 && NBOOST <= NMAX && I0 % 2 == 0 && I0 >= 2 && 4 * I0 <= NMAX, "fixed by the loader code");
__global__ void __launch_bounds__(704, 1) k_main(Args a) {
  __shared__ __attribute__((aligned(16))) char smem[L_END];
  LAS char* L = (LAS char*)smem;
  const int tid = threadIdx.x, lane = tid & 63, w = RFL(tid >> 6), r = lane >> 4, c = lane & 15;
  const int b = blockIdx.x, xg = b & 7, idx = b >> 3;
  const int head = 4 * xg + (idx >> 3), split = idx & 7;
  volatile LAS int* landed = (volatile LAS int*)(L + L_FL);
  volatile LAS int* ctl = (volatile LAS int*)(L + L_CT);
  const unsigned tstart = (unsigned)__builtin_amdgcn_s_memrealtime();
  if (w == 10) { ctl[lane] = 0; landed[lane] = 0; landed[64 + lane] = 0; }
  BARRIER();
  {
    const char* p = a.k + (size_t)head * (SK * DH * 2) + (split << CHB) + (w & 1) * 4096;
    const char* pv = a.v + (size_t)head * (SK * DH * 2) + (split << CHB) + (w & 1) * 4096;
#define PIECE_ADDR "s_mov_b32 m0, s87\n\ts_add_u32 s80, s84, s86\n\ts_addc_u32 s81, s85, 0\n\t"
#define PIECE_NEXT "s_add_u32 s86, s86, %c[inc]\n\ts_and_b32 s86, s86, %c[msk]\n\ts_add_u32 s87, s87, 0x2000\n\ts_and_b32 s87, s87, 0x1ffff\n\t"
#define PIECE PIECE_ADDR "s_swappc_b64 s[98:99], s[96:97]\n\tds_write_b32 v141, v142\n\tv_add_u32 v142, 1, v142\n\t" PIECE_NEXT
#define LOAD4 "global_load_lds_dwordx4 v140, s[80:81] nt\n\tglobal_load_lds_dwordx4 v140, s[80:81] offset:1024 nt\n\t" \
              "global_load_lds_dwordx4 v140, s[80:81] offset:2048 nt\n\tglobal_load_lds_dwordx4 v140, s[80:81] offset:3072 nt\n\t"
#define BLK(n) "s_waitcnt vmcnt(" #n ")\n\tglobal_load_lds_dwordx4 v140, s[80:81] nt\n\t" \
               "s_waitcnt vmcnt(" #n ")\n\tglobal_load_lds_dwordx4 v140, s[80:81] offset:1024 nt\n\t" \
               "s_waitcnt vmcnt(" #n ")\n\tglobal_load_lds_dwordx4 v140, s[80:81] offset:2048 nt\n\t" \
               "s_waitcnt vmcnt(" #n ")\n\tglobal_load_lds_dwordx4 v140, s[80:81] offset:3072 nt\n\t" \
               "s_setpc_b64 s[98:99]\n\t"
#define TAIL(left) "s_waitcnt vmcnt(4 * " #left ")\n\tv_mov_b32 v142, 256 - " #left "\n\tds_write_b32 v141, v142\n\t"
    asm volatile(
        "s_cmp_gt_u32 %[w], 1\n\t"
        "s_cbranch_scc1 99f\n\t"
        "; loader entry\n\t"
        "s_setprio 3\n\t"
        "s_mov_b64 s[84:85], %[p]\n\t"
        "s_mov_b32 s86, %[head]\n\t"
        "s_lshl_b32 s87, %[w], 12\n\t"
        "v_lshlrev_b32 v140, 4, %[lane]\n\t"
        "s_mov_b32 s88, 0\n\t"
        "1:\n\t"
        PIECE_ADDR LOAD4 PIECE_NEXT
        "s_add_u32 s88, s88, 1\n\t"
        "s_cmp_lt_u32 s88, %c[i0]\n\t"
        "s_cbranch_scc1 1b\n\t"
        "s_memrealtime s[98:99]\n\t"
        "s_lshl_b32 s82, %[w], 8\n\t"
        "s_add_u32 s82, s82, %c[lfl]\n\t"
        "v_lshl_add_u32 v141, %[lane], 2, s82\n\t"
        "s_lshl_b32 s82, %[w], 2\n\t"
        "s_add_u32 s82, s82, %c[lct]\n\t"
        "v_mov_b32 v143, s82\n\t"
        "v_mov_b32 v144, %c[lct]\n\t"
        "s_mov_b32 s89, 16\n\t"
        "s_mov_b32 s91, 0\n\t"
        "s_mov_b32 s79, 0\n\t"
        "s_mov_b32 s92, %c[acc0]\n\t"
        "s_lshl_b32 s93, %[w], 7\n\t"
        "s_add_u32 s93, s93, %c[n0]\n\t"
        "s_getpc_b64 s[94:95]\n\t"
        "2:\n\t"
        "s_add_u32 s94, s94, 7f - 2b\n\t"
        "s_addc_u32 s95, s95, 0\n\t"
        "s_waitcnt lgkmcnt(0)\n\t"
        "s_mov_b32 s90, s98\n\t"
        "3:\n\t"
        "s_lshr_b32 s82, s92, 8\n\t"
        "s_sub_u32 s83, s90, s82\n\t"
        "v_mov_b32 v145, s83\n\t"
        "s_sub_u32 s82, s83, s91\n\t"
        "s_min_i32 s82, s82, s79\n\t"
        "s_mul_i32 s82, s82, %c[kq]\n\t"
        "s_ashr_i32 s82, s82, 8\n\t"
        "s_cmp_eq_u32 s91, 0\n\t"
        "s_cselect_b32 s82, 0, s82\n\t"
        "s_add_u32 s82, s82, s93\n\t"
        "s_ashr_i32 s82, s82, 8\n\t"
        "s_max_i32 s82, s82, %c[nmin]\n\t"
        "s_min_i32 s82, s82, %c[nmax]\n\t"
        "s_cmp_lt_u32 s88, %c[jb]\n\t"
        "s_cselect_b32 s82, %c[nb], s82\n\t"
        "s_mul_i32 s83, s82, 52\n\t"
        "s_sub_u32 s83, s83, 8 * 52\n\t"
        "s_add_u32 s96, s94, s83\n\t"
        "s_addc_u32 s97, s95, 0\n\t"
        "s_add_u32 s82, s82, 3\n\t"
        "s_lshr_b32 s82, s82, 2\n\t"
        "s_sub_u32 s82, s88, s82\n\t"
        "s_add_u32 s82, s82, 1\n\t"
        "v_mov_b32 v142, s82\n\t"
        "s_cmp_ge_i32 s88, s89\n\t"
        "s_cbranch_scc1 5f\n\t"
        "4:\n\t"
        "ds_write_b32 v143, v145 offset:80\n\t"
        "ds_read_b64 v[146:147], v144 offset:96\n\t"
        "s_memrealtime s[82:83]\n\t"
        PIECE
        PIECE
        "s_add_u32 s88, s88, 2\n\t"
        "s_add_u32 s92, s92, %c[tau2]\n\t"
        "s_cmp_eq_u32 s88, 128\n\t"
        "s_cselect_b64 s[84:85], %[pv], s[84:85]\n\t"
        "s_waitcnt lgkmcnt(0)\n\t"
        "s_mov_b32 s90, s82\n\t"
        "v_readfirstlane_b32 s91, v146\n\t"
        "v_readfirstlane_b32 s79, v147\n\t"
        "s_cmp_lt_u32 s88, 0x100\n\t"
        "s_cbranch_scc1 3b\n\t"
        TAIL(5) TAIL(4) TAIL(3) TAIL(2) TAIL(1) TAIL(0)
        "s_endpgm\n\t"
        "5:\n\t"
        "s_mov_b32 s83, %c[spin]\n\t"
        "6:\n\t"
        "ds_read2_b32 v[146:147], v143 offset1:2\n\t"
        "s_waitcnt lgkmcnt(0)\n\t"
        "v_min_i32 v146, v146, v147\n\t"
        "s_nop 0\n\t"
        "v_readfirstlane_b32 s89, v146\n\t"
        "s_lshl_b32 s89, s89, 1\n\t"
        "s_add_u32 s89, s89, 16\n\t"
        "s_cmp_lt_i32 s88, s89\n\t"
        "s_cbranch_scc1 4b\n\t"
        "s_sleep 1\n\t"
        "s_sub_u32 s83, s83, 1\n\t"
        "s_cbranch_scc0 6b\n\t"
        "s_mov_b32 s89, 0x7fffffff\n\t"
        "s_branch 4b\n\t"
        "7:\n\t"
        BLK(7) BLK(8) BLK(9) BLK(10) BLK(11) BLK(12) BLK(13) BLK(14) BLK(15) BLK(16) BLK(17) BLK(18) BLK(19) BLK(20) BLK(21) BLK(22) BLK(23)
        "99:"
        :: [w] "s"(w), [p] "s"(p), [pv] "s"(pv), [head] "s"((head << 18)), [lane] "v"(lane), [i0] "i"(I0), [inc] "i"(8192 + (7 << CHB)), [msk] "i"(0x7fffff & ~(7 << CHB)), [lfl] "i"(L_FL), [lct] "i"(L_CT),
           [acc0] "i"((I0 - 2) * TAU_Q), [tau2] "i"(2 * TAU_Q), [n0] "i"(N0_16 * 16), [kq] "i"(SCHED_Q * KP16 / 16), [nmin] "i"(NMIN), [nmax] "i"(NMAX),
           [jb] "i"(JBOOST), [nb] "i"(NBOOST), [spin] "i"(LDS_SPIN)
        : "memory", "scc", "s79", "s80", "s81", "s82", "s83", "s84", "s85", "s86", "s87", "s88", "s89", "s90", "s91", "s92", "s93", "s94", "s95", "s96", "s97",
          "s98", "s99", "v140", "v141", "v142", "v143", "v144", "v145", "v146", "v147");
#undef PIECE_ADDR
#undef PIECE_NEXT
#undef PIECE
#undef LOAD4
#undef BLK
#undef TAIL
  }
  v4u xr = {}, wt[4] = {};
  v2u x4 = {};
  if (w >= 2 && w <= 5) {
    const int row = 128 * (split >> 1) + 32 * (w - 2) + 4 * (lane >> 3);
    const GAS char* qp = (const GAS char*)a.wq + (size_t)row * (NH * DH * 2) + head * (DH * 2) + (split & 1) * 128 + (lane & 7) * 16;
#pragma unroll
    for (int i = 0; i < 4; ++i) wt[i] = ldw(qp + i * (NH * DH * 2));
    xr = ldg((const GAS char*)a.x + lane * 16);
    x4 = *(const GAS v2u*)((const GAS char*)a.x + row * 2);
  }
  SB();

  const __amdgpu_buffer_rsrc_t rs = __builtin_amdgcn_make_buffer_rsrc(a.ws, 0, W_END, 0x00020000);
  unsigned tag;
  asm volatile("s_load_dword %0, %1, 0x0" : "=s"(tag) : "s"(a.ws) : "memory");
  {
    PIN(xr);
    float mb, rden;
    ln_stats(xr, mb, rden);
    f32x2 qa[4] = {};
    fma8(qa, ln_elem(bflo(x4[0]), mb, rden), wt[0]);
    fma8(qa, ln_elem(bfhi(x4[0]), mb, rden), wt[1]);
    fma8(qa, ln_elem(bflo(x4[1]), mb, rden), wt[2]);
    fma8(qa, ln_elem(bfhi(x4[1]), mb, rden), wt[3]);
    const float y[8] = {qa[0].x, qa[0].y, qa[1].x, qa[1].y, qa[2].x, qa[2].y, qa[3].x, qa[3].y};
    *(volatile LAS f32x2*)(L + L_QP + (w - 2) * 512 + lane * 8) = xrow_tr8(y);
    asm volatile("s_waitcnt lgkmcnt(0)" : "+s"(tag) :: "memory");
    ctl[32 + w - 2] = 1;
  }

  if (w <= 5) {
    const int cw = w - 2;
    v4u q = {};
    constexpr float SCALE = 0.088388347648318440f, LOG2E = 1.4426950408889634f;
    LAS float* sc = (LAS float*)(L + L_SC) + cw * 1024 + lane;
    int known = 0;
    auto fetch = [&](int n, v4u (&t)[4]) {
      const int j = 4 * n + cw;
      wait_lds<1>(landed + 64 * (cw & 1), (j >> 1) + 1, known);
      const LAS v4u* src = (const LAS v4u*)(L + (j & (RING - 1)) * 4096) + lane;
#pragma unroll
      for (int k = 0; k < 4; ++k) t[k] = src[64 * k];
      asm volatile("s_waitcnt lgkmcnt(0)" : "+v"(t[0]), "+v"(t[1]), "+v"(t[2]), "+v"(t[3]) :: "memory");
      ctl[cw] = n + 1;
      MEMB();
    };
    float m = -INFINITY;
#pragma unroll 1
    for (int g = 0; g < 16; g += 2) {
      v4u t[8][4];
#pragma unroll
      for (int pi = 0; pi < 8; ++pi) fetch(4 * g + pi, t[pi]);
      if (g == 0) {
        int kq = 0;
        wait_lds<1>(ctl + 16, 1, kq);
        q = *(const LAS v4u*)(L + L_Q + c * 16);
      }
#pragma unroll
      for (int gg = 0; gg < 2; ++gg) {
        float z[4];
#pragma unroll
        for (int pi = 0; pi < 4; ++pi) {
          float x[4];
#pragma unroll
          for (int k = 0; k < 4; ++k) { const v4u tt = t[4 * gg + pi][k]; x[k] = dot2(tt[3], q[3], dot2(tt[2], q[2], dot2(tt[1], q[1], dot2(tt[0], q[0], 0.f)))); }
          z[pi] = comb<XOR7>(comb<XOR8>(x[0], x[1], c & 8), comb<XOR8>(x[2], x[3], c & 8), c & 4);
        }
        float s = comb<XOR1>(comb<XOR2>(z[0], z[1], c & 2), comb<XOR2>(z[2], z[3], c & 2), c & 1);
        s = rbf(rbf(s) * SCALE) * LOG2E;
        m = fmaxf(m, s);
        sc[(g + gg) * 64] = s;
      }
    }
    m = xrow_max(row_max(m));
    {
      float l = 0.f;
#pragma unroll
      for (int g = 0; g < 16; ++g) { const float p = __builtin_amdgcn_exp2f(sc[g * 64] - m); l += p; sc[g * 64] = p; }
      l = xrow_sum(row_sum(l));
      *(volatile LAS f32x2*)(L + L_ML + cw * 8) = f32x2{m, l};
      asm volatile("s_waitcnt lgkmcnt(0)" ::: "memory");
      ctl[4 + cw] = 1;
    }
    f32x2 acc[4] = {};
#pragma unroll 1
    for (int g = 0; g < 16; ++g) {
      const float pv = sc[g * 64];
      Unroll<4>::run([&]<int pi>() {
        v4u t[4];
        fetch(64 + 4 * g + pi, t);
        Unroll<4>::run([&]<int k>() { fma8(acc, dpp<BCAST + rev4(4 * pi + k)>(pv), t[k]); });
        PIN(acc[0]); PIN(acc[1]); PIN(acc[2]); PIN(acc[3]);
      });
    }
    {
      const float y[8] = {acc[0].x, acc[0].y, acc[1].x, acc[1].y, acc[2].x, acc[2].y, acc[3].x, acc[3].y};
      *(LAS f32x2*)(L + L_OP + cw * 512 + lane * 8) = xrow_tr8(y);
    }
    BARRIER();
    __builtin_amdgcn_endpgm();
  }

  const bool pass = w != 10;
  const int hw = pass ? w - 6 : 0;
  tag += 1u;
  const bool reducer = split == 2;
  const int rid = head;
  const int ocol = 128 * (rid >> 3) + 64 * (rid & 1) + 8 * (lane & 7) + 2 * ((rid >> 1) & 3);
  const int wx = head & 15;
  unsigned x2 = 0;
  const __amdgpu_buffer_rsrc_t ro = __builtin_amdgcn_make_buffer_rsrc(a.out, 0, DM * 2, 0x00020000);
  unsigned wp[16][8];
  float es[4] = {};
  f32x2 yf2 = {};
  if (pass) {
    if (hw < 2) {
      if (hw == 0) wait_flags4<1>(ctl + 32);
      f32x2 pp = {};
#pragma unroll
      for (int k = 0; k < 4; ++k) pp += *(const LAS f32x2*)(L + L_QP + k * 512 + lane * 8);
      pp.x += dpp<XOR8>(pp.x); pp.y += dpp<XOR8>(pp.y);
      const int qo = W_QG + head * 4096;
      __builtin_amdgcn_raw_buffer_store_b128(v4u{fu(pp.x), tag, fu(pp.y), tag}, rs, hw == 0 && c < 8 ? qo + split * 512 + r * 128 + c * 16 : OOB, 0, AUX_SC1);
      int off[4];
      f32x2 val[4] = {};
#pragma unroll
      for (int i = 0; i < 4; ++i) off[i] = hw == 0 ? qo + i * 1024 + lane * 16 : OOB;
      if (hw == 0) __builtin_amdgcn_s_sleep(40);
      sweep<4, 12>(rs, off, tag, val);
      const f32x2 qs = (val[0] + val[1]) + (val[2] + val[3]);
      *(volatile LAS unsigned*)(L + L_Q + hw * 256 + ((lane & 32) + 4 * (lane & 7) + ((lane >> 3) & 3)) * 4) = packbf(qs.x, qs.y);
      asm volatile("s_waitcnt lgkmcnt(0)" ::: "memory");
      ctl[16 + hw] = 1;
    }
    int known = 0;
    wait_lds<8>(landed, HSTART, known);
    if (hw == 1 && b < NFS) {
      const int mh = b / 43, li = b - 43 * mh, pr = lane >> 3, ch = lane & 7;
      const int m0 = 128 * (mh & 1) + 16 * pr;
      const GAS char* src = (const GAS char*)(mh < 2 ? a.wg : a.wu) + m0 * (4 * FF) + li * 128 + ch * 16;
      const bool odd = li * 8 + ch >= NFS;
      v4u xq[4], t[16];
      xr = ldg((const GAS char*)a.x + lane * 16);
#pragma unroll
      for (int i = 0; i < 4; ++i) xq[i] = ldg((const GAS char*)a.x + m0 * 4 + i * 16);
#pragma unroll
      for (int i = 0; i < 16; ++i) t[i] = ldw(src + i * (4 * FF));
      SB();
      float mb, rden;
      ln_stats(xr, mb, rden);
      f32x2 ga[4] = {};
#pragma unroll
      for (int i = 0; i < 16; ++i) fma8(ga, ln_elem(odd ? bfhi(xq[i >> 2][i & 3]) : bflo(xq[i >> 2][i & 3]), mb, rden), t[i]);
      const float yg[8] = {ga[0].x, ga[0].y, ga[1].x, ga[1].y, ga[2].x, ga[2].y, ga[3].x, ga[3].y};
      f32x2 g2 = xrow_tr8(yg);
      g2.x += dpp<XOR8>(g2.x); g2.y += dpp<XOR8>(g2.y);
      if (c < 8) __builtin_amdgcn_raw_buffer_store_b128(v4u{fu(g2.x), tag, fu(g2.y), tag}, rs, W_FG + (b * 4 + r) * 128 + c * 16, 0, AUX_SC1);
    }
    if (b >= NWG - NFS) {
      const int fs = b - (NWG - NFS);
      wait_lds<16>(landed, FSTART, known);
      const GAS char* dp = (const GAS char*)a.wd + (size_t)(fs * 8 + r) * (DM * 2) + hw * 256 + c * 16;
      const v4u d0 = ldw(dp), d1 = ldw(dp + 4 * (DM * 2));
      if (hw == 1) {
        const int cidx = fs + ((lane >> 2) & 1) * NFS;
        int off[1] = {lane < 32 ? W_FG + (((lane >> 3) * 43 + (cidx >> 3)) * 4 + (lane & 3)) * 128 + (cidx & 7) * 16 : OOB};
        f32x2 val[1] = {};
        sweep<1, 32>(rs, off, tag, val);
        float e[2] = {val[0].x, val[0].y}, hv[2];
#pragma unroll
        for (int k = 0; k < 2; ++k) {
          e[k] += dpp<ROR4>(e[k]); e[k] += dpp<XOR8>(e[k]);
          auto sw = __builtin_amdgcn_permlane16_swap(fu(e[k]), fu(e[k]), false, false);
          const float gv = rbf(uf(sw[0])), uv = rbf(uf(sw[1]));
          const float inner = 0.7978845608028654f * (gv + 0.044715f * gv * gv * gv);
          const float ge = rbf(gv * __builtin_amdgcn_rcpf(1.f + __builtin_amdgcn_exp2f(-2.885390081777927f * inner)));
          hv[k] = rbf(ge * uv);
        }
        if (lane < 4) *(volatile LAS f32x2*)(L + L_H + lane * 8) = f32x2{hv[0], hv[1]};
        asm volatile("s_waitcnt lgkmcnt(0)" ::: "memory");
        ctl[12] = 1;
      }
      int kh = 0;
      wait_lds<8>(ctl + 12, 1, kh);
      f32x2 yf[4] = {};
      fma8(yf, *(const LAS float*)(L + L_H + r * 4), d0);
      fma8(yf, *(const LAS float*)(L + L_H + 16 + r * 4), d1);
      const float y[8] = {yf[0].x, yf[0].y, yf[1].x, yf[1].y, yf[2].x, yf[2].y, yf[3].x, yf[3].y};
      yf2 = xrow_tr8(y);
    }
    PIN(yf2);
    {
      const GAS char* wop = (const GAS char*)a.wo + (size_t)(head * DH + 2 * r) * (DM * 2) + hw * 256 + c * 16;
#pragma unroll
      for (int u = 0; u < 16; ++u) {
        wait_until(tstart + WT0 + u * WDT + head * WDT / 32 + (split == (u & 7) ? 0 : WLEAD));
        const GAS char* wr = wop + (u ^ wx) * (8 * DM * 2);
        const v4u A = ldw(wr), B = ldw(wr + DM * 2);
        SB();
#pragma unroll
        for (int k = 0; k < 4; ++k) { wp[u][2 * k] = __builtin_amdgcn_perm(B[k], A[k], 0x05040100u); wp[u][2 * k + 1] = __builtin_amdgcn_perm(B[k], A[k], 0x07060302u); }
#pragma unroll
        for (int k = 0; k < 8; ++k) PIN(wp[u][k]);
        SB();
      }
    }
    {
      wait_flags4<32>(ctl + 4);
      const f32x4 ml0 = *(const LAS f32x4*)(L + L_ML), ml1 = *(const LAS f32x4*)(L + L_ML + 16);
      const float M = fmaxf(fmaxf(ml0[0], ml0[2]), fmaxf(ml1[0], ml1[2]));
      const float Ls = (__builtin_amdgcn_exp2f(ml0[0] - M) * ml0[1] + __builtin_amdgcn_exp2f(ml0[2] - M) * ml0[3]) +
                   (__builtin_amdgcn_exp2f(ml1[0] - M) * ml1[1] + __builtin_amdgcn_exp2f(ml1[2] - M) * ml1[3]);
      if (hw == 0 && lane == 0) __builtin_amdgcn_raw_buffer_store_b128(v4u{fu(M), tag, fu(Ls), tag}, rs, W_LS + (head * 8 + split) * 16, 0, AUX_SC1);
      wait_lds<32>(landed, RSTART, known);
      int off[1] = {W_LS + (head * 8 + (lane & 7)) * 16};
      f32x2 val[1] = {};
      sweep<1, 32>(rs, off, tag, val);
      float MH = fmaxf(val[0].x, dpp<XOR7>(val[0].x)); MH = fmaxf(MH, dpp<XOR2>(MH)); MH = fmaxf(MH, dpp<XOR1>(MH));
      float LH = __builtin_amdgcn_exp2f(val[0].x - MH) * val[0].y;
      LH += dpp<XOR7>(LH); LH += dpp<XOR2>(LH); LH += dpp<XOR1>(LH);
      const float inv = __builtin_amdgcn_rcpf(LH);
      es[0] = uf(RFL(fu(__builtin_amdgcn_exp2f(ml0[0] - MH) * inv))); es[1] = uf(RFL(fu(__builtin_amdgcn_exp2f(ml0[2] - MH) * inv)));
      es[2] = uf(RFL(fu(__builtin_amdgcn_exp2f(ml1[0] - MH) * inv))); es[3] = uf(RFL(fu(__builtin_amdgcn_exp2f(ml1[2] - MH) * inv)));
    }
    x2 = *(const GAS unsigned*)((const GAS char*)a.x + ocol * 2);
    PIN(x2);
    BARRIER();
  } else {
    int kn = 0;
    {
      wait_lds<8>(landed, 1, kn);
      const size_t hb = (size_t)head * (SK * DH * 2);
      const int fo = b & 8 ? DM * FF * 2 - 4 : 0;
      unsigned d[12];
      asm volatile("s_load_dword %0, %12, 0x0\n\ts_load_dword %1, %12, %18\n\ts_load_dword %2, %12, %19\n\ts_load_dword %3, %12, %20\n\t"
                   "s_load_dword %8, %14, 0x0\n\ts_load_dword %9, %15, 0x0\n\ts_load_dword %10, %16, 0x0\n\ts_load_dword %11, %17, 0x0\n\t"
                   "s_load_dword %4, %13, 0x0\n\ts_load_dword %5, %13, %18\n\ts_load_dword %6, %13, %19\n\ts_load_dword %7, %13, %20\n\t"
                   "s_waitcnt lgkmcnt(0)"
                   : "=&s"(d[0]), "=&s"(d[1]), "=&s"(d[2]), "=&s"(d[3]), "=&s"(d[4]), "=&s"(d[5]), "=&s"(d[6]), "=&s"(d[7]), "=&s"(d[8]), "=&s"(d[9]),
                     "=&s"(d[10]), "=&s"(d[11])
                   : "s"(a.k + hb), "s"(a.v + hb), "s"(a.wg + fo), "s"(a.wu + fo), "s"(a.wd + fo), "s"(a.wo + head * (DH * DM * 2)),
                     "s"(2 << 20), "s"(4 << 20), "s"(6 << 20) : "memory");
    }
#pragma unroll 1
    for (int k = 0; k < PC_N; ++k) {
      wait_lds<16>(landed, PC_FIRST + k * PC_STEP, kn);
      const unsigned own = ctl[20];
      if ((idx & 15) == 5 && lane == 0) __builtin_amdgcn_raw_buffer_store_b64(v2u{own, tag}, rs, W_PC + k * 128 + (xg * 2 + (idx >> 4)) * 8, 0, AUX_SC1);
      wait_lds<32>(landed, PC_FIRST + k * PC_STEP + PC_DLY, kn);
      const v2u g = __builtin_amdgcn_raw_buffer_load_b64(rs, W_PC + k * 128 + c * 8, 0, AUX_SC1);
      const bool ok = g[1] == tag;
      const float sum = row_sum(ok ? (float)(int)(g[0] - own) : 0.f), cnt = row_sum(ok ? 1.f : 0.f);
      const int dm = (int)(sum * __builtin_amdgcn_rcpf(cnt));
      if (cnt >= 12.f) *(volatile LAS v2u*)(ctl + 24) = v2u{(own + dm) | 1u, (unsigned)(VCAP - dm)};
    }
#pragma unroll
    for (int u = 0; u < 16; ++u)
#pragma unroll
      for (int k = 0; k < 8; ++k) asm volatile("" : "=v"(wp[u][k]));
  }
  {
    f32x2 o = {};
#pragma unroll
    for (int k = 0; k < 4; ++k) o = __builtin_elementwise_fma(f32x2{es[k], es[k]}, *(const LAS f32x2*)(L + L_OP + k * 512 + (lane ^ wx) * 8), o);
    const unsigned op = packbf(o.x, o.y);
    float y[8] = {};
    Unroll<16>::run([&]<int u>() {
      const unsigned ob = dppu<BCAST + u>(op);
#pragma unroll
      for (int k = 0; k < 8; ++k) y[k] = dot2(ob, wp[u][k], y[k]);
    });
    const f32x2 yy = xrow_tr8(y) + yf2;
    __builtin_amdgcn_raw_buffer_store_b128(v4u{fu(yy.x), tag, fu(yy.y), tag}, rs, pass ? W_YA + ((8 * hw + (lane >> 3)) * 256 + b) * 128 + (lane & 7) * 16 : OOB, 0, AUX_SC1);
  }
  if (reducer) {
    int off[8];
    f32x2 val[8] = {};
#pragma unroll
    for (int i = 0; i < 8; ++i) off[i] = pass ? W_YA + (rid * 256 + 64 * hw) * 128 + i * 1024 + lane * 16 : OOB;
    __builtin_amdgcn_s_sleep(16);
    sweep<8, 0>(rs, off, tag, val);
    const f32x2 A = ((val[0] + val[4]) + (val[2] + val[6])) + ((val[1] + val[5]) + (val[3] + val[7]));
    const float As = xrow_sum(comb<XOR8>(A.x, A.y, c & 8));
    if (lane < 16) *(volatile LAS float*)(L + L_RD + lane * 16 + hw * 4) = As;
    if (pass) BARRIER();
    MEMB();
    if (hw == 0) {
      const f32x4 pa = *(const LAS f32x4*)(L + L_RD + c * 16);
      const float o = ((c & 8) ? bfhi(x2) : bflo(x2)) + ((pa[0] + pa[1]) + (pa[2] + pa[3]));
      const float o1 = dpp<XOR8>(o);
      __builtin_amdgcn_raw_buffer_store_b32(packbf(o, o1), ro, pass && lane < 8 ? ocol * 2 : OOB, 0, 0);
      __builtin_amdgcn_raw_buffer_store_b32(tag, rs, pass && rid == 0 && lane == 0 ? W_EP : OOB, 0, AUX_SC1);
    }
  }
}

extern "C" void kernel_launch(void* const* d_in, const int* in_sizes, int n_in, void* d_out, int out_size, void* d_ws, size_t ws_size,
                              hipStream_t stream) {
  Args a;
  a.x = (const char*)d_in[0]; a.k = (const char*)d_in[1]; a.v = (const char*)d_in[2]; a.wq = (const char*)d_in[3]; a.wo = (const char*)d_in[4];
  a.wg = (const char*)d_in[6]; a.wu = (const char*)d_in[7]; a.wd = (const char*)d_in[8]; a.ws = (char*)d_ws; a.out = (char*)d_out;
  k_main<<<NWG, 704, 0, stream>>>(a);
}
```
